```python
import torch

def _softmax(x, axis=-1):
    return torch.softmax(x, dim=axis)
def _gelu(x):
    return torch.nn.functional.gelu(x, approximate="tanh")
def _silu(x):
    return torch.nn.functional.silu(x)
def _sigmoid(x):
    return torch.sigmoid(x)
def _layernorm(x, axis=-1, eps=1e-5):
    m = x.mean(dim=axis, keepdim=True)
    v = x.var(dim=axis, keepdim=True, unbiased=False)
    return (x - m) / torch.sqrt(v + eps)
def _scatter_add(tgt, idx, src, axis=0):
    return tgt.clone().index_add_(axis, idx, src)
def _segment_sum(src, seg, n):
    out = torch.zeros((n,) + src.shape[1:], dtype=src.dtype)
    return out.index_add_(0, seg, src)
def _pad(x, pw):
    flat = tuple(v for pair in reversed(pw) for v in pair)
    return torch.nn.functional.pad(x, flat)

kernel_name = 'standalone__grn__sampling'

def setup_inputs():

    _w = (lambda a: (lambda t: t.to(torch.bfloat16) if t.is_floating_point() else t)(torch.as_tensor(a)))
    B, S, D = 1024, 32, 512
    return {
        "x": _w((lambda *s: torch.randn(*s, dtype=torch.bfloat16) * 0.05)(B, S, D)),
        "g": _w((lambda *s: torch.randn(*s, dtype=torch.bfloat16) * 0.4)(D) * 0.1),
        "b": _w((lambda *s: torch.randn(*s, dtype=torch.bfloat16) * 0.1)(D) * 0.1),
    }

def reference(x, g, b):
    gx = torch.sqrt((x * x).sum(axis=-2, keepdims=True))
    nx = gx / (gx.mean(axis=-1, keepdims=True) + 1e-6)
    return g * (x * nx) + b + x
```

```cpp
#include <hip/hip_runtime.h>
#include <hip/hip_bf16.h>

typedef unsigned int u32;
typedef u32 u32x4 __attribute__((ext_vector_type(4)));
typedef float f32x4 __attribute__((ext_vector_type(4)));
typedef float f32x2 __attribute__((ext_vector_type(2)));
typedef __bf16 bf16x2_t __attribute__((ext_vector_type(2)));

constexpr int S_ = 32;
constexpr int D_ = 512;

constexpr u32 kM[15] = {0x009, 0x084, 0x060, 0x001, 0x1de, 0x114, 0x304, 0x200, 0x100, 0x007,
                        0x008, 0x800, 0x967, 0x4a0, 0x0a8};
constexpr u32 kR[3] = {0x04, 0x17, 0x18};
constexpr u32 kL[2] = {0x11, 0x10};
constexpr int kS[3] = {4, 1, 2};

constexpr u32 map_col(int j) {
    u32 c = 0;
    for (int k = 0; k < 10; ++k) c |= ((kM[k] >> j) & 1u) << (15 + k);
    for (int k = 0; k < 5; ++k) c |= ((kM[10 + k] >> j) & 1u) << (10 + k);
    return c;
}
constexpr u32 slot_off(int r) {
    return (((r & 1) ? kR[0] : 0u) ^ ((r & 2) ? kR[1] : 0u) ^ ((r & 4) ? kR[2] : 0u)) << 10;
}
constexpr bool map_is_bijective() {
    bool seen[1024] = {};
    for (u32 b = 0; b < 1024; ++b) {
        u32 y = 0;
        for (int j = 0; j < 10; ++j) y ^= ((b >> j) & 1u) ? map_col(j) : 0u;
        if (seen[y >> 15]) return false;
        seen[y >> 15] = true;
    }
    if ((map_col(10) | map_col(11)) >> 15) return false;
    u32 rows = 0;
    for (int w = 0; w < 4; ++w)
        for (int r = 0; r < 8; ++r)
            rows |= 1u << ((((w & 1) ? map_col(10) : 0u) ^ ((w & 2) ? map_col(11) : 0u) ^ slot_off(r)) >> 10);
    u32 ord = 0;
    for (int i = 0; i < 8; ++i) ord |= 1u << (((i & 1) ? kS[0] : 0) ^ ((i & 2) ? kS[1] : 0) ^ ((i & 4) ? kS[2] : 0));
    return rows == 0xffffffffu && ord == 0xffu && kL[0] < 32 && kL[1] < 32;
}
static_assert(map_is_bijective(), "address map must cover every (batch, row) exactly once");

__device__ __forceinline__ float lo_f(u32 v) { return __uint_as_float(v << 16); }
__device__ __forceinline__ float hi_f(u32 v) { return __uint_as_float(v & 0xffff0000u); }
__device__ __forceinline__ u32 pack_bf16(float a, float b) {
    f32x2 t = {a, b};
    bf16x2_t r = __builtin_convertvector(t, bf16x2_t);
    return __builtin_bit_cast(u32, r);
}
template <int CTRL>
__device__ __forceinline__ float dpp_add(float v) {
    int t = __builtin_amdgcn_update_dpp(0, __float_as_int(v), CTRL, 0xf, 0xf, true);
    return v + __int_as_float(t);
}
__device__ __forceinline__ float wave_sum(float v) {
    v = dpp_add<0xB1>(v);
    v = dpp_add<0x4E>(v);
    v = dpp_add<0x141>(v);
    v = dpp_add<0x140>(v);
    float a = __int_as_float(__builtin_amdgcn_readlane(__float_as_int(v), 0));
    float b = __int_as_float(__builtin_amdgcn_readlane(__float_as_int(v), 16));
    float c = __int_as_float(__builtin_amdgcn_readlane(__float_as_int(v), 32));
    float d = __int_as_float(__builtin_amdgcn_readlane(__float_as_int(v), 48));
    return (a + b) + (c + d);
}

template <bool PERM>
__device__ __forceinline__ void grn_body(const u32x4* __restrict__ x, const u32x4* __restrict__ g,
                                         const u32x4* __restrict__ b, u32x4* __restrict__ out) {
    __shared__ f32x4 red[4 * 2 * 64];
    __builtin_amdgcn_s_setprio(3);
    const u32 tid = threadIdx.x;
    const u32 lane = tid & 63;
    const u32 bid = blockIdx.x;

    u32 so = 0;
    if (PERM) {
#pragma unroll
        for (int j = 0; j < 10; ++j) so ^= ((bid >> j) & 1u) ? map_col(j) : 0u;
    } else {
        x += (size_t)bid * (S_ * D_ / 8);
        out += (size_t)bid * (S_ * D_ / 8);
    }
    u32 vo = lane << 4;
    vo ^= (tid & 16) ? kL[0] << 10 : 0u;
    vo ^= (tid & 32) ? kL[1] << 10 : 0u;
    vo ^= (tid & 64) ? map_col(10) : 0u;
    vo ^= (tid & 128) ? map_col(11) : 0u;
    vo ^= so;
    u32 off[8];
#pragma unroll
    for (int r = 0; r < 8; ++r) off[r] = vo ^ slot_off(r);

    u32x4 v[8];
#pragma unroll
    for (int r = 0; r < 8; ++r) v[r] = __builtin_nontemporal_load((const u32x4*)((const char*)x + off[r]));
    const u32x4 gv = g[lane];
    const u32x4 bv = b[lane];
    __builtin_amdgcn_sched_barrier(0);
    __builtin_amdgcn_s_setprio(0);
    __builtin_amdgcn_sched_barrier(0);

    float ss[8];
#pragma unroll
    for (int i = 0; i < 8; ++i) ss[i] = 0.f;
#pragma unroll
    for (int r = 0; r < 8; ++r) {
#pragma unroll
        for (int i = 0; i < 4; ++i) {
            const float a = lo_f(v[r][i]), c = hi_f(v[r][i]);
            ss[2 * i] = fmaf(a, a, ss[2 * i]);
            ss[2 * i + 1] = fmaf(c, c, ss[2 * i + 1]);
        }
    }
    red[(tid >> 6) * 128 + lane] = f32x4{ss[0], ss[1], ss[2], ss[3]};
    red[(tid >> 6) * 128 + 64 + lane] = f32x4{ss[4], ss[5], ss[6], ss[7]};
    __syncthreads();
    f32x4 t0 = red[lane], t1 = red[64 + lane];
#pragma unroll
    for (int w = 1; w < 4; ++w) {
        t0 += red[w * 128 + lane];
        t1 += red[w * 128 + 64 + lane];
    }
    float gx[8], tot = 0.f;
#pragma unroll
    for (int i = 0; i < 4; ++i) {
        gx[i] = __builtin_amdgcn_sqrtf(t0[i]);
        gx[4 + i] = __builtin_amdgcn_sqrtf(t1[i]);
    }
#pragma unroll
    for (int i = 0; i < 8; ++i) tot += gx[i];
    tot = wave_sum(tot);
    const float inv = 1.0f / (tot * (1.0f / D_) + 1e-6f);
    float sc[8], bb[8];
#pragma unroll
    for (int i = 0; i < 4; ++i) {
        sc[2 * i] = fmaf(lo_f(gv[i]), gx[2 * i] * inv, 1.0f);
        sc[2 * i + 1] = fmaf(hi_f(gv[i]), gx[2 * i + 1] * inv, 1.0f);
        bb[2 * i] = lo_f(bv[i]);
        bb[2 * i + 1] = hi_f(bv[i]);
    }
    const __amdgpu_buffer_rsrc_t orr =
        __builtin_amdgcn_make_buffer_rsrc((void*)out, 0, PERM ? (1u << 25) : (1u << 15), 0x00020000);
#pragma unroll
    for (int i8 = 0; i8 < 8; ++i8) {
        const int r = ((i8 & 1) ? kS[0] : 0) ^ ((i8 & 2) ? kS[1] : 0) ^ ((i8 & 4) ? kS[2] : 0);
        u32x4 o;
#pragma unroll
        for (int i = 0; i < 4; ++i)
            o[i] = pack_bf16(fmaf(lo_f(v[r][i]), sc[2 * i], bb[2 * i]),
                             fmaf(hi_f(v[r][i]), sc[2 * i + 1], bb[2 * i + 1]));
        __builtin_amdgcn_raw_buffer_store_b128(o, orr, off[r], 0, 16  );
        __builtin_amdgcn_sched_barrier(0);
    }
}

__global__ __launch_bounds__(256, 4) __attribute__((aligned(4096))) void grn_kernel(
    const u32x4* __restrict__ x, const u32x4* __restrict__ g, const u32x4* __restrict__ b,
    u32x4* __restrict__ out) {
    grn_body<true>(x, g, b, out);
}
__global__ __launch_bounds__(256, 4) void grn_kernel_any_batch(
    const u32x4* __restrict__ x, const u32x4* __restrict__ g, const u32x4* __restrict__ b,
    u32x4* __restrict__ out) {
    grn_body<false>(x, g, b, out);
}

extern "C" void kernel_launch(void* const* d_in, const int* in_sizes, int n_in,
                              void* d_out, int out_size, void* d_ws, size_t ws_size,
                              hipStream_t stream) {
    const int B = in_sizes[0] / (S_ * D_);
    const u32x4* x = (const u32x4*)d_in[0];
    const u32x4* g = (const u32x4*)d_in[1];
    const u32x4* b = (const u32x4*)d_in[2];
    if (B == 1024)
        grn_kernel<<<B, 256, 0, stream>>>(x, g, b, (u32x4*)d_out);
    else
        grn_kernel_any_batch<<<B, 256, 0, stream>>>(x, g, b, (u32x4*)d_out);
}
```
